```python
import torch

def _softmax(x, axis=-1):
    return torch.softmax(x, dim=axis)
def _gelu(x):
    return torch.nn.functional.gelu(x, approximate="tanh")
def _silu(x):
    return torch.nn.functional.silu(x)
def _sigmoid(x):
    return torch.sigmoid(x)
def _layernorm(x, axis=-1, eps=1e-5):
    m = x.mean(dim=axis, keepdim=True)
    v = x.var(dim=axis, keepdim=True, unbiased=False)
    return (x - m) / torch.sqrt(v + eps)
def _scatter_add(tgt, idx, src, axis=0):
    return tgt.clone().index_add_(axis, idx, src)
def _segment_sum(src, seg, n):
    out = torch.zeros((n,) + src.shape[1:], dtype=src.dtype)
    return out.index_add_(0, seg, src)
def _pad(x, pw):
    flat = tuple(v for pair in reversed(pw) for v in pair)
    return torch.nn.functional.pad(x, flat)

kernel_name = 'standalone__kv_cache_quant_int4_int2_kv_with_per_channel_or_per_token_scales__decode'

def setup_inputs():

    _w = (lambda a: (lambda t: t.to(torch.bfloat16) if t.is_floating_point() else t)(torch.as_tensor(a)))
    B = 2
    Sq = 1
    Skv = 32768
    Hq = 32
    Hkv = 16
    Dh = 128
    bits = 2
    G = -1
    if G <= 0 or G >= Skv:
        G = max(1, Skv // 2)
    Skv = (Skv // G) * G
    ngk = Skv // G
    qmax = (1 << bits) - 1
    return {
        "q": _w((lambda *s: torch.randn(*s, dtype=torch.bfloat16) * 3.2)(B, Hq, Sq, Dh)),
        "qk": _w(torch.randint(0, qmax + 1, (B, Hkv, Skv, Dh)).to(torch.int32)),
        "qv": _w(torch.randint(0, qmax + 1, (B, Hkv, Skv, Dh)).to(torch.int32)),
        "k_scales": _w(torch.abs((lambda *s: torch.randn(*s, dtype=torch.bfloat16) * 3.2)(B, Hkv, ngk, Dh)) * 0.05 + 0.02),
        "k_zeros": _w((lambda *s: torch.randn(*s, dtype=torch.bfloat16) * 3.2)(B, Hkv, ngk, Dh) + float(qmax) / 2.0),
        "v_scales": _w(torch.abs((lambda *s: torch.randn(*s, dtype=torch.bfloat16) * 0.05)(B, Hkv, Skv, 1)) * 0.05 + 0.02),
        "v_zeros": _w((lambda *s: torch.randn(*s, dtype=torch.bfloat16) * 0.05)(B, Hkv, Skv, 1) + float(qmax) / 2.0),
    }

def reference(q, qk, qv, k_scales, k_zeros, v_scales, v_zeros):
    B, Hq, Sq, Dh = q.shape
    Hkv, Skv = qk.shape[1], qk.shape[2]
    ngk = k_scales.shape[2]
    G = Skv // ngk
    ks = (lambda x, n, axis: torch.repeat_interleave(x, n, dim=axis))(k_scales, G, axis=2)
    kz = (lambda x, n, axis: torch.repeat_interleave(x, n, dim=axis))(k_zeros, G, axis=2)
    k = ((lambda x, dt: x.to(dt))(qk, q.dtype) - kz) * ks
    v = ((lambda x, dt: x.to(dt))(qv, q.dtype) - v_zeros) * v_scales
    rep = Hq // Hkv
    if rep > 1:
        k = (lambda x, n, axis: torch.repeat_interleave(x, n, dim=axis))(k, rep, axis=1)
        v = (lambda x, n, axis: torch.repeat_interleave(x, n, dim=axis))(v, rep, axis=1)
    scale = float(Dh) ** -0.5
    logits = torch.einsum("bhqd,bhkd->bhqk", q, k) * scale
    p = _softmax(logits, axis=-1)
    o = torch.einsum("bhqk,bhkd->bhqd", p, v)
    return o
```

```cpp
#include <hip/hip_runtime.h>
#include <hip/hip_bf16.h>
#include <stdint.h>

namespace {

constexpr int B_ = 2, HQ = 32, HKV = 16, SKV = 32768, DH = 128, NGK = 2;
constexpr int REP = HQ / HKV;
constexpr int GTOK = SKV / NGK;
constexpr int NBH = B_ * HKV;
#define NSPLIT 32
#define NWAVE 4
constexpr int NTHR = NWAVE * 64;
constexpr int TOK_WG = SKV / NSPLIT;
constexpr int BT = 16;
constexpr int NBATCH = TOK_WG / (BT * NWAVE);
static_assert(REP == 2 && GTOK % TOK_WG == 0 && TOK_WG % (BT * NWAVE) == 0, "shape");
constexpr int PREC = 132;

typedef int i32x4 __attribute__((ext_vector_type(4)));
typedef unsigned u32x4 __attribute__((ext_vector_type(4)));
typedef unsigned u32x2 __attribute__((ext_vector_type(2)));
typedef float f32x2 __attribute__((ext_vector_type(2)));
typedef float f32x4 __attribute__((ext_vector_type(4)));

__device__ __forceinline__ float bf_lo(unsigned u) { return __uint_as_float(u << 16); }
__device__ __forceinline__ float bf_hi(unsigned u) { return __uint_as_float(u & 0xffff0000u); }

template <int CTRL> __device__ __forceinline__ float dpp(float x) {
  return __builtin_bit_cast(float, __builtin_amdgcn_mov_dpp(__builtin_bit_cast(int, x), CTRL, 0xf, 0xf, true));
}
constexpr int XOR1 = 0xB1, XOR2 = 0x4E, XOR7 = 0x141, XOR8 = 0x128, ROR4 = 0x124;
__device__ __forceinline__ float x16_sum(float x) {
  auto s = __builtin_amdgcn_permlane16_swap(__float_as_uint(x), __float_as_uint(x), false, false);
  return __uint_as_float(s[0]) + __uint_as_float(s[1]);
}
__device__ __forceinline__ float x32_sum(float x) {
  auto s = __builtin_amdgcn_permlane32_swap(__float_as_uint(x), __float_as_uint(x), false, false);
  return __uint_as_float(s[0]) + __uint_as_float(s[1]);
}

__global__ void __launch_bounds__(NTHR)
kv_split_kernel(const unsigned short* __restrict__ q, const int* __restrict__ qk, const int* __restrict__ qv,
                const unsigned short* __restrict__ ksc, const unsigned short* __restrict__ kzr,
                const unsigned short* __restrict__ vsc, const unsigned short* __restrict__ vzr,
                float* __restrict__ part) {
  __shared__ __attribute__((aligned(16))) float lds[NWAVE * NBATCH * BT * 2 + NWAVE * 2 * 2 * PREC];
  const int tid = threadIdx.x, lane = tid & 63;
  const int wave = __builtin_amdgcn_readfirstlane(tid >> 6);
  const int bh = blockIdx.x / NSPLIT, split = blockIdx.x % NSPLIT;
  const int half = lane >> 5, c = lane & 31, i16 = lane & 15;
  const int tok0 = split * TOK_WG;
  const int grp = tok0 / GTOK;

  const u32x2 q0 = *reinterpret_cast<const u32x2*>(q + (size_t)(bh * REP + 0) * DH + 4 * c);
  const u32x2 q1 = *reinterpret_cast<const u32x2*>(q + (size_t)(bh * REP + 1) * DH + 4 * c);
  const u32x2 ks = *reinterpret_cast<const u32x2*>(ksc + (size_t)(bh * NGK + grp) * DH + 4 * c);
  const u32x2 kz = *reinterpret_cast<const u32x2*>(kzr + (size_t)(bh * NGK + grp) * DH + 4 * c);
  constexpr int NSV = (NBATCH * 2 + 63) / 64;
  u32x4 vs8[NSV], vz8[NSV];
#pragma unroll
  for (int u = 0; u < NSV; ++u) {
    const int hb = u * 64 + lane;
    const int hbc = hb < NBATCH * 2 ? hb : 0;
    const size_t o = (size_t)bh * SKV + tok0 + ((hbc >> 1) * NWAVE + wave) * BT + (hbc & 1) * 8;
    vs8[u] = *reinterpret_cast<const u32x4*>(vsc + o);
    vz8[u] = *reinterpret_cast<const u32x4*>(vzr + o);
  }

  const size_t base = ((size_t)bh * SKV + tok0 + wave * BT) * DH + lane * 4;
  const i32x4* kp = reinterpret_cast<const i32x4*>(qk + base);
  const i32x4* vp = reinterpret_cast<const i32x4*>(qv + base);
  constexpr int BSTRIDE = NWAVE * BT * DH / 4;
  i32x4 K[8], V[8];
#pragma unroll
  for (int j = 0; j < 8; ++j) K[j] = __builtin_nontemporal_load(kp + j * 64);
#pragma unroll
  for (int j = 0; j < 8; ++j) V[j] = __builtin_nontemporal_load(vp + j * 64);

  constexpr float C2 = 0.08838834764831845f * 1.4426950408889634f;
  float a0[4], a1[4];
  {
    const float s0 = bf_lo(ks.x) * C2, s1 = bf_hi(ks.x) * C2, s2 = bf_lo(ks.y) * C2, s3 = bf_hi(ks.y) * C2;
    a0[0] = bf_lo(q0.x) * s0; a0[1] = bf_hi(q0.x) * s1; a0[2] = bf_lo(q0.y) * s2; a0[3] = bf_hi(q0.y) * s3;
    a1[0] = bf_lo(q1.x) * s0; a1[1] = bf_hi(q1.x) * s1; a1[2] = bf_lo(q1.y) * s2; a1[3] = bf_hi(q1.y) * s3;
  }
  float cz0, cz1;
  {
    const float z0 = bf_lo(kz.x), z1 = bf_hi(kz.x), z2 = bf_lo(kz.y), z3 = bf_hi(kz.y);
    cz0 = a0[0] * z0 + a0[1] * z1 + a0[2] * z2 + a0[3] * z3;
    cz1 = a1[0] * z0 + a1[1] * z1 + a1[2] * z2 + a1[3] * z3;
    cz0 += dpp<XOR1>(cz0); cz0 += dpp<XOR2>(cz0); cz0 += dpp<XOR7>(cz0); cz0 += dpp<XOR8>(cz0); cz0 = x16_sum(cz0);
    cz1 += dpp<XOR1>(cz1); cz1 += dpp<XOR2>(cz1); cz1 += dpp<XOR7>(cz1); cz1 += dpp<XOR8>(cz1); cz1 = x16_sum(cz1);
  }
  float* sv = lds + wave * (NBATCH * BT * 2);
#pragma unroll
  for (int u = 0; u < NSV; ++u) {
    const int hb = u * 64 + lane;
    if (hb < NBATCH * 2) {
      f32x4* d = reinterpret_cast<f32x4*>(sv + hb * 16);
#pragma unroll
      for (int e = 0; e < 4; ++e) d[e] = f32x4{bf_lo(vs8[u][e]), bf_lo(vz8[u][e]), bf_hi(vs8[u][e]), bf_hi(vz8[u][e])};
    }
  }
  __builtin_amdgcn_wave_barrier();

  float m = -INFINITY, l = 0.f, z = 0.f;
  float acc0[4] = {0.f, 0.f, 0.f, 0.f}, acc1[4] = {0.f, 0.f, 0.f, 0.f};
  const f32x2* svl = reinterpret_cast<const f32x2*>(sv) + (i16 >> 1) * 2 + half;

  for (int it = 0; it < NBATCH; ++it) {
    const f32x2 sz = svl[it * BT];
    const bool more = it + 1 < NBATCH;
    const int nxt = (more ? it + 1 : it) * BSTRIDE;
    float x[16];
#pragma unroll
    for (int j = 0; j < 8; ++j) {
      const float k0 = (float)K[j][0], k1 = (float)K[j][1], k2 = (float)K[j][2], k3 = (float)K[j][3];
      x[2 * j + 0] = fmaf(a0[3], k3, fmaf(a0[2], k2, fmaf(a0[1], k1, a0[0] * k0)));
      x[2 * j + 1] = fmaf(a1[3], k3, fmaf(a1[2], k2, fmaf(a1[1], k1, a1[0] * k0)));
    }
    if (more) {
#pragma unroll
      for (int j = 0; j < 8; ++j) K[j] = __builtin_nontemporal_load(kp + nxt + j * 64);
    }
#define TR_STEP(HALF, CTRL, BIT) _Pragma("unroll") for (int h = 0; h < (HALF); ++h) { \
      const float lo_ = x[h], hi_ = x[h + (HALF)]; const float t1_ = lo_ + dpp<CTRL>(lo_), t2_ = hi_ + dpp<CTRL>(hi_); \
      x[h] = (i16 & (BIT)) ? t2_ : t1_; }
    TR_STEP(8, XOR8, 8) TR_STEP(4, XOR7, 4) TR_STEP(2, XOR2, 2) TR_STEP(1, XOR1, 1)
#undef TR_STEP
    const float s = x16_sum(x[0]);
    float bm = fmaxf(s, dpp<XOR2>(s)); bm = fmaxf(bm, dpp<ROR4>(bm)); bm = fmaxf(bm, dpp<XOR8>(bm));
    const float mn = fmaxf(m, bm);
    const float alpha = __builtin_amdgcn_exp2f(m - mn);
    const float p = __builtin_amdgcn_exp2f(s - mn);
    m = mn;
    const float w = p * sz.x;
    l = fmaf(l, alpha, p);
    z = fmaf(z, alpha, w * sz.y);
    {
      const float al0 = dpp<0x00>(alpha), al1 = dpp<0x55>(alpha);
#pragma unroll
      for (int e = 0; e < 4; ++e) { acc0[e] *= al0; acc1[e] *= al1; }
    }
#define PV(j) { const float w0 = dpp<0x150 + 2 * (j)>(w), w1 = dpp<0x151 + 2 * (j)>(w); \
      _Pragma("unroll") for (int e = 0; e < 4; ++e) { const float vf = (float)V[j][e]; acc0[e] = fmaf(w0, vf, acc0[e]); acc1[e] = fmaf(w1, vf, acc1[e]); } }
    PV(0) PV(1) PV(2) PV(3) PV(4) PV(5) PV(6) PV(7)
#undef PV
    if (more) {
#pragma unroll
      for (int j = 0; j < 8; ++j) V[j] = __builtin_nontemporal_load(vp + nxt + j * 64);
    }
  }

  l += dpp<XOR2>(l); l += dpp<ROR4>(l); l += dpp<XOR8>(l);
  z += dpp<XOR2>(z); z += dpp<ROR4>(z); z += dpp<XOR8>(z);
  const float z0 = dpp<0x00>(z), z1 = dpp<0x55>(z);
  float* rec = lds + NWAVE * NBATCH * BT * 2 + ((wave * 2 + half) * 2) * PREC;
  *reinterpret_cast<f32x4*>(rec + 4 + 4 * c) = f32x4{acc0[0] - z0, acc0[1] - z0, acc0[2] - z0, acc0[3] - z0};
  *reinterpret_cast<f32x4*>(rec + PREC + 4 + 4 * c) = f32x4{acc1[0] - z1, acc1[1] - z1, acc1[2] - z1, acc1[3] - z1};
  if (c < 2) { rec[c * PREC + 0] = m - (c ? cz1 : cz0); rec[c * PREC + 1] = l; }
  __syncthreads();
  if (tid < 2 * DH) {
    const int h = tid >> 7, d = tid & 127;
    const float* r0 = lds + NWAVE * NBATCH * BT * 2 + h * PREC;
    float M = -INFINITY;
#pragma unroll
    for (int u = 0; u < NWAVE * 2; ++u) M = fmaxf(M, r0[u * 2 * PREC]);
    float L = 0.f, o = 0.f;
#pragma unroll
    for (int u = 0; u < NWAVE * 2; ++u) {
      const float wt = __builtin_amdgcn_exp2f(r0[u * 2 * PREC] - M);
      L = fmaf(wt, r0[u * 2 * PREC + 1], L);
      o = fmaf(wt, r0[u * 2 * PREC + 4 + d], o);
    }
    float* pr = part + ((size_t)(bh * REP + h) * NSPLIT + split) * PREC;
    pr[4 + d] = o;
    if (d == 0) { pr[0] = M; pr[1] = L; }
  }
}

__global__ void __launch_bounds__(128)
kv_combine_kernel(const float* __restrict__ part, __hip_bfloat16* __restrict__ out) {
  const int row = blockIdx.x, d = threadIdx.x;
  const float* pr = part + (size_t)row * NSPLIT * PREC;
  float ms[NSPLIT], ls[NSPLIT], os[NSPLIT];
#pragma unroll
  for (int s = 0; s < NSPLIT; ++s) { ms[s] = pr[s * PREC]; ls[s] = pr[s * PREC + 1]; os[s] = pr[s * PREC + 4 + d]; }
  float M = -INFINITY;
#pragma unroll
  for (int s = 0; s < NSPLIT; ++s) M = fmaxf(M, ms[s]);
  float L = 0.f, o = 0.f;
#pragma unroll
  for (int s = 0; s < NSPLIT; ++s) {
    const float wt = __builtin_amdgcn_exp2f(ms[s] - M);
    L = fmaf(wt, ls[s], L);
    o = fmaf(wt, os[s], o);
  }
  out[(size_t)row * DH + d] = __float2bfloat16(o / L);
}

}

extern "C" void kernel_launch(void* const* d_in, const int* in_sizes, int n_in,
                              void* d_out, int out_size, void* d_ws, size_t ws_size,
                              hipStream_t stream) {
  (void)in_sizes; (void)n_in; (void)out_size; (void)ws_size;
  float* part = (float*)d_ws;
  kv_split_kernel<<<NBH * NSPLIT, NTHR, 0, stream>>>(
      (const unsigned short*)d_in[0], (const int*)d_in[1], (const int*)d_in[2],
      (const unsigned short*)d_in[3], (const unsigned short*)d_in[4],
      (const unsigned short*)d_in[5], (const unsigned short*)d_in[6], part);
  kv_combine_kernel<<<B_ * HQ, 128, 0, stream>>>(part, (__hip_bfloat16*)d_out);
}
```

```cpp
#define TPACE 13650
#include <hip/hip_runtime.h>
#include <hip/hip_bf16.h>
#include <stdint.h>

#define POFF 150
#define PMUL 5
#define SKIPTHR 80
#define LINGTHR 5
#define LINGN 6
#define ENDREL 107
#define DRY 1
#define HSTAGE 1
#define HSLEEP 2
#define BARIT 24
#define FUSE 1

namespace {

constexpr int B_ = 2, HQ = 32, HKV = 16, SKV = 32768, DH = 128, NGK = 2;
constexpr int REP = HQ / HKV;
constexpr int GTOK = SKV / NGK;
constexpr int NBH = B_ * HKV;
constexpr int NSPLIT = 8;
constexpr int NWAVE = 4, NTHR = NWAVE * 64;
constexpr int REG_TOK = SKV / NSPLIT;
constexpr int NIT = REG_TOK / 64;
constexpr unsigned ROWB = DH * 4;
constexpr unsigned PIECEB = 1u << 15;
constexpr unsigned REGB = REG_TOK * ROWB;
__host__ __device__ constexpr unsigned it_off(unsigned it) { return ((it << 14) & (PIECEB - 1)) | (((it << 14) & ~(PIECEB - 1)) << 1); }
static_assert(REP == 2 && GTOK % REG_TOK == 0 && NIT == 64, "shape");
constexpr int PREC = 132;
constexpr int LDS_VS = 0, LDS_VZ = REG_TOK / 2, LDS_T0 = REG_TOK, LDS_SC = REG_TOK + 4, LDS_MRG = LDS_SC + NWAVE * NIT * 32,
              LDS_FLOATS = LDS_MRG + (NWAVE + 1) * 2 * PREC;
constexpr int LDSB = 84 * 1024;
static_assert(LDS_FLOATS * 4 + 2048 <= LDSB, "lds");
constexpr int GSTAT = NSPLIT * 256, GCHK = GSTAT + 32, GTEAM = GCHK + 32;
constexpr size_t WS_GRAN = 65536;

typedef int i32x4 __attribute__((ext_vector_type(4)));
typedef unsigned u32x4 __attribute__((ext_vector_type(4)));
typedef unsigned u32x2 __attribute__((ext_vector_type(2)));
typedef float f32x4 __attribute__((ext_vector_type(4)));
typedef __bf16 bf16x2 __attribute__((ext_vector_type(2)));
typedef unsigned long long u64;
typedef __attribute__((address_space(1))) u64 gu64;
typedef __attribute__((address_space(1))) unsigned gu32;

__device__ __forceinline__ float bf_lo(unsigned u) { return __uint_as_float(u << 16); }
__device__ __forceinline__ float bf_hi(unsigned u) { return __uint_as_float(u & 0xffff0000u); }
__device__ __forceinline__ float rbf(float x) {
  float r; asm("v_cvt_pk_bf16_f32 %0, 0, %1" : "=v"(r) : "v"(x)); return r;
}
__device__ __forceinline__ unsigned pkbf(float lo, float hi) {
  unsigned r; asm("v_cvt_pk_bf16_f32 %0, %1, %2" : "=v"(r) : "v"(lo), "v"(hi)); return r;
}
__device__ __forceinline__ float dot2(unsigned a, unsigned b, float c) {
  return __builtin_amdgcn_fdot2_f32_bf16(__builtin_bit_cast(bf16x2, a), __builtin_bit_cast(bf16x2, b), c, false);
}
template <int CTRL> __device__ __forceinline__ float dpp(float x) {
  return __builtin_bit_cast(float, __builtin_amdgcn_mov_dpp(__builtin_bit_cast(int, x), CTRL, 0xf, 0xf, true));
}
constexpr int XOR1 = 0xB1, XOR2 = 0x4E, XOR7 = 0x141, XOR8 = 0x128, ROR4 = 0x124, BCAST = 0x150;
__device__ __forceinline__ float x16_sum(float x) {
  auto s = __builtin_amdgcn_permlane16_swap(__float_as_uint(x), __float_as_uint(x), false, false);
  return __uint_as_float(s[0]) + __uint_as_float(s[1]);
}
__device__ __forceinline__ float x32_sum(float x) {
  auto s = __builtin_amdgcn_permlane32_swap(__float_as_uint(x), __float_as_uint(x), false, false);
  return __uint_as_float(s[0]) + __uint_as_float(s[1]);
}
__device__ __forceinline__ float x32_max(float x) {
  auto s = __builtin_amdgcn_permlane32_swap(__float_as_uint(x), __float_as_uint(x), false, false);
  return fmaxf(__uint_as_float(s[0]), __uint_as_float(s[1]));
}
__device__ __forceinline__ unsigned wave_xor(unsigned x) {
  x ^= (unsigned)__builtin_amdgcn_mov_dpp((int)x, XOR1, 0xf, 0xf, true); x ^= (unsigned)__builtin_amdgcn_mov_dpp((int)x, XOR2, 0xf, 0xf, true);
  x ^= (unsigned)__builtin_amdgcn_mov_dpp((int)x, XOR7, 0xf, 0xf, true); x ^= (unsigned)__builtin_amdgcn_mov_dpp((int)x, XOR8, 0xf, 0xf, true);
  return __builtin_amdgcn_readlane(x, 0) ^ __builtin_amdgcn_readlane(x, 16) ^ __builtin_amdgcn_readlane(x, 32) ^ __builtin_amdgcn_readlane(x, 48);
}
__device__ __forceinline__ i32x4 ldc(__amdgpu_buffer_rsrc_t r, unsigned voff, unsigned soff) {
  return __builtin_bit_cast(i32x4, __builtin_amdgcn_raw_buffer_load_b128(r, voff, soff, 2  ));
}
__device__ __forceinline__ unsigned clk() { return (unsigned)__builtin_amdgcn_s_memrealtime(); }
__device__ __forceinline__ void st_gran(gu64* g, float v, unsigned tag) {
  __hip_atomic_store(g, ((u64)tag << 32) | __float_as_uint(v), __ATOMIC_RELAXED, __HIP_MEMORY_SCOPE_AGENT);
}
#define PIN16(a, o) asm volatile("" : "+v"(a[o + 0][0]), "+v"(a[o + 0][1]), "+v"(a[o + 0][2]), "+v"(a[o + 0][3]), "+v"(a[o + 1][0]), "+v"(a[o + 1][1]), "+v"(a[o + 1][2]), "+v"(a[o + 1][3]), \
                                      "+v"(a[o + 2][0]), "+v"(a[o + 2][1]), "+v"(a[o + 2][2]), "+v"(a[o + 2][3]), "+v"(a[o + 3][0]), "+v"(a[o + 3][1]), "+v"(a[o + 3][2]), "+v"(a[o + 3][3]) :: "memory")

template <int J> __device__ __forceinline__ void pv_step(const float (&vf)[8][4], float w, float (&acc0)[4], float (&acc1)[4]) {
  if constexpr (J < 8) {
    const float w0 = dpp<BCAST + 2 * J>(w), w1 = dpp<BCAST + 2 * J + 1>(w);
#pragma unroll
    for (int e = 0; e < 4; ++e) { acc0[e] = fmaf(w0, vf[J][e], acc0[e]); acc1[e] = fmaf(w1, vf[J][e], acc1[e]); }
    pv_step<J + 1>(vf, w, acc0, acc1);
  }
}

__global__ void __launch_bounds__(NTHR + 64)
kv_decode_kernel(const unsigned short* __restrict__ q, const int* __restrict__ qk, const int* __restrict__ qv,
                 const unsigned short* __restrict__ ksc, const unsigned short* __restrict__ kzr,
                 const unsigned short* __restrict__ vsc, const unsigned short* __restrict__ vzr,
                 unsigned char* ws, __hip_bfloat16* __restrict__ out) {
  extern __shared__ __attribute__((aligned(16))) float lds[];
#define RT(x)
  const unsigned tent = clk();
  const int bid = __builtin_amdgcn_readfirstlane(blockIdx.x);
  const int tid = threadIdx.x, lane = tid & 63;
  const int wave = __builtin_amdgcn_readfirstlane(tid >> 6);
  const int bh = (unsigned)bid / NSPLIT, split = (unsigned)bid % NSPLIT;
  const int half = lane >> 5, c = lane & 31, i16 = lane & 15;
  const int grp = (unsigned)split / (GTOK / REG_TOK);

#define EWORD ((gu32*)(ws + bh * 128))
#define GTEAM_PTR ((gu64*)(ws + WS_GRAN) + (size_t)bh * GTEAM)
  float mx = -INFINITY;
  double dl = 0.0, dz = 0.0, da0[4] = {0.0, 0.0, 0.0, 0.0}, da1[4] = {0.0, 0.0, 0.0, 0.0};
  if (wave == NWAVE) {
    __syncthreads();
    {
      u32x4 sv[16];
#pragma unroll
      for (int u = 0; u < 16; ++u) sv[u] = __builtin_nontemporal_load(reinterpret_cast<const u32x4*>((u < 8 ? vsc : vzr) + (size_t)bid * REG_TOK + ((u & 7) * 64 + lane) * 8));
#pragma unroll
      for (int u = 0; u < 16; ++u) *reinterpret_cast<u32x4*>(lds + LDS_VS + (u * 64 + lane) * 4) = sv[u];
    }
    for (int i = 0; i < HSLEEP; ++i) __builtin_amdgcn_s_sleep(127);
    unsigned epoch, touch;
    asm volatile("s_load_dword %0, %2, 0x0\n\ts_load_dword %1, %3, 0x0\n\ts_waitcnt lgkmcnt(0)" : "=&s"(epoch), "=&s"(touch)
                 : "s"(ws + bh * 128), "s"(qv + (size_t)bid * (REG_TOK * DH)) : "memory");
    lds[LDS_T0 + 2] = __uint_as_float(epoch);
    __syncthreads();
  } else {

  const __amdgpu_buffer_rsrc_t rk = __builtin_amdgcn_make_buffer_rsrc((void*)(qk + (size_t)bid * (REG_TOK * DH)), 0, REGB, 0x00020000);
  const __amdgpu_buffer_rsrc_t rv = __builtin_amdgcn_make_buffer_rsrc((void*)(qv + (size_t)bid * (REG_TOK * DH)), 0, REGB, 0x00020000);
  unsigned vo = lane * 16;
  const unsigned so0 = wave * (8 * ROWB);
  const unsigned phi = (((unsigned)bid * PMUL) & 255u) * TPACE / 256u;
  unsigned cur = ((((tent + POFF + 25u) & 0xffffffu) * 128u + phi) / TPACE) & (NIT - 1);
  u64 rem = ~(1ull << cur);
  i32x4 A[4], Bq[4], F0[8];
#pragma unroll
  for (int j = 0; j < 8; ++j) { F0[j] = ldc(rk, vo + (j & 3) * 1024, so0 + it_off(cur) + (j >> 2) * PIECEB); __builtin_amdgcn_sched_barrier(0); }
  if (wave == 0) { lds[LDS_T0] = __uint_as_float(clk()); lds[LDS_T0 + 1] = 0.f; }
  __builtin_amdgcn_sched_barrier(0);
  const u32x2 q0 = *reinterpret_cast<const u32x2*>(q + (size_t)(bh * REP + 0) * DH + 4 * c);
  const u32x2 q1 = *reinterpret_cast<const u32x2*>(q + (size_t)(bh * REP + 1) * DH + 4 * c);
  const u32x2 ks = *reinterpret_cast<const u32x2*>(ksc + (size_t)(bh * NGK + grp) * DH + 4 * c);
  const u32x2 kz = *reinterpret_cast<const u32x2*>(kzr + (size_t)(bh * NGK + grp) * DH + 4 * c);
  __builtin_amdgcn_sched_barrier(0);
  __builtin_amdgcn_sched_barrier(0);

  __syncthreads();
  const unsigned t0 = __builtin_amdgcn_readfirstlane(__float_as_uint(lds[LDS_T0]));
  unsigned due, frac, slot;
  {
    const unsigned s0 = t0 + POFF;
    const unsigned x = (s0 & 0xffffffu) * 128u + phi, r = x % TPACE;
    slot = x / TPACE;
    due = s0 - (r + 127u) / 128u;
    frac = (128u - (r & 127u)) & 127u;
  }
#define REC()
  unsigned kslip = 0, linger = 0;
  unsigned* kword = reinterpret_cast<unsigned*>(lds + LDS_T0 + 1);
  const unsigned tend = t0 + POFF + TPACE - ENDREL;
#define ADV() { frac += TPACE; due += frac / 128u; frac %= 128u; ++slot; }
  auto pick = [&]() -> unsigned {
    const unsigned i = slot & (NIT - 1);
    const u64 rot = (rem >> i) | ((rem << 1) << (63u - i));
    const unsigned j = (rot & 1ull) ? i : (i + 63u - (unsigned)__builtin_clzll(rot)) & (NIT - 1);
    rem &= ~(1ull << j);
    return j;
  };
#define PACE() { ADV() \
    const unsigned kk = __builtin_amdgcn_readfirstlane(__hip_atomic_load(kword, __ATOMIC_RELAXED, __HIP_MEMORY_SCOPE_WORKGROUP)); \
    unsigned now = clk(); \
    while (kslip < kk) { ADV() ++kslip; } \
    REC() \
    linger = (int)(now - due) > LINGTHR ? linger + 1 : 0; \
    if ((int)(now - due) > SKIPTHR || linger >= LINGN) { \
      do { ADV() ++kslip; } while ((int)(now - due) > SKIPTHR); \
      linger = 0; \
      if (lane == 0) __hip_atomic_fetch_max(kword, kslip, __ATOMIC_RELAXED, __HIP_MEMORY_SCOPE_WORKGROUP); \
    } \
    nxt = pick(); \
    son = so0 + it_off(nxt); \
    asm volatile("" : "+s"(son)); \
    const unsigned dw = (int)(due - tend) > 0 ? tend : due; \
    while ((int)(now - dw) < 0) { __builtin_amdgcn_s_sleep(1); now = clk(); } }
#define DRAIN(F, O, P) \
    __builtin_amdgcn_sched_barrier(0); \
    _Pragma("unroll") for (int j = 0; j < 4; ++j) _Pragma("unroll") for (int e = 0; e < 4; ++e) { F[O + j][e] = (float)P[j][e]; } \
    PIN16(F, O); \
    __builtin_amdgcn_sched_barrier(0);
#define FETCH(P, RS, SO) \
    asm volatile("" : "+v"(vo)); \
    __builtin_amdgcn_sched_barrier(0); \
    _Pragma("unroll") for (int j = 0; j < 4; ++j) { P[j] = ldc(RS, vo + j * 1024, SO); __builtin_amdgcn_sched_barrier(0); }

  unsigned nxt, son;
  float f[8][4];
  PACE()
  FETCH(A, rk, son)
  FETCH(Bq, rk, son + PIECEB)
#pragma unroll
  for (int j = 0; j < 8; ++j)
#pragma unroll
    for (int e = 0; e < 4; ++e) f[j][e] = (float)F0[j][e];

  constexpr float SCALE = 0.08838834764831845f, LOG2E = 1.4426950408889634f;
  const float kzf[4] = {bf_lo(kz.x), bf_hi(kz.x), bf_lo(kz.y), bf_hi(kz.y)};
  const float ksf[4] = {bf_lo(ks.x), bf_hi(ks.x), bf_lo(ks.y), bf_hi(ks.y)};
  float* scw = lds + LDS_SC + wave * (NIT * 32) + half * 16 + i16;
#pragma unroll 1
  for (int it = 0; it < NIT; ++it) {
    float x[16];
#pragma unroll
    for (int j = 0; j < 8; ++j) {
      const unsigned k01 = pkbf(rbf(f[j][0] - kzf[0]) * ksf[0], rbf(f[j][1] - kzf[1]) * ksf[1]);
      const unsigned k23 = pkbf(rbf(f[j][2] - kzf[2]) * ksf[2], rbf(f[j][3] - kzf[3]) * ksf[3]);
      x[2 * j + 0] = dot2(k23, q0.y, dot2(k01, q0.x, 0.f));
      x[2 * j + 1] = dot2(k23, q1.y, dot2(k01, q1.x, 0.f));
    }
#define TR_STEP(HALF, CTRL, BIT) _Pragma("unroll") for (int h = 0; h < (HALF); ++h) { \
      const float lo_ = x[h], hi_ = x[h + (HALF)]; const float t1_ = lo_ + dpp<CTRL>(lo_), t2_ = hi_ + dpp<CTRL>(hi_); \
      x[h] = (i16 & (BIT)) ? t2_ : t1_; }
    TR_STEP(8, XOR8, 8) TR_STEP(4, XOR7, 4) TR_STEP(2, XOR2, 2) TR_STEP(1, XOR1, 1)
#undef TR_STEP
    const float s = rbf(rbf(x16_sum(x[0])) * SCALE) * LOG2E;
    mx = fmaxf(mx, s);
    scw[cur * 32] = s;
    if (it == BARIT) __syncthreads();
    const bool nv = it + 2 >= NIT;
    const __amdgpu_buffer_rsrc_t rn = __builtin_amdgcn_make_buffer_rsrc((void*)((nv ? qv : qk) + (size_t)bid * (REG_TOK * DH)), 0, REGB, 0x00020000);
    if (it + 2 == NIT) rem = ~0ull;
    DRAIN(f, 0, A)
    cur = nxt;
    PACE()
    FETCH(A, rn, son)
    DRAIN(f, 4, Bq)
    FETCH(Bq, rn, son + PIECEB)
  }
  mx = fmaxf(mx, dpp<XOR2>(mx)); mx = fmaxf(mx, dpp<ROR4>(mx)); mx = fmaxf(mx, dpp<XOR8>(mx)); mx = x32_max(mx);

  const unsigned short* svl = reinterpret_cast<const unsigned short*>(lds + LDS_VS) + (i16 >> 3) * (PIECEB / ROWB) + wave * 8 + ((i16 >> 1) & 3) * 2 + half;
#pragma unroll 1
  for (int it = 0; it < NIT; ++it) {
    const float s = scw[cur * 32];
    const float vs = bf_lo(svl[it_off(cur) / ROWB]), vz = bf_lo(svl[it_off(cur) / ROWB + REG_TOK]);
    const float p = __builtin_amdgcn_exp2f(s - mx);
    const float w = p * vs;
    dl += (double)p;
    dz += (double)(w * vz);
    float t0_[4] = {0.f, 0.f, 0.f, 0.f}, t1_[4] = {0.f, 0.f, 0.f, 0.f};
    pv_step<0>(f, w, t0_, t1_);
#pragma unroll
    for (int e = 0; e < 4; ++e) { da0[e] += (double)t0_[e]; da1[e] += (double)t1_[e]; }
    asm volatile("" : "+v"(da0[0]), "+v"(da0[1]), "+v"(da0[2]), "+v"(da0[3]), "+v"(da1[0]), "+v"(da1[1]), "+v"(da1[2]), "+v"(da1[3]) :: "memory");
    if (it == NIT - 1) break;
    DRAIN(f, 0, A)
    cur = nxt;
    vo |= (unsigned)((it + 2) / NIT) << 31;
    if (it + 2 < NIT) PACE()
    son = __builtin_amdgcn_readfirstlane(son);
    FETCH(A, rv, son)
    DRAIN(f, 4, Bq)
    FETCH(Bq, rv, son + PIECEB)
  }
#undef DRAIN
#undef FETCH
#undef PACE
#undef ADV
  }
  RT(tf)
  int wv = wave;
  asm volatile("" : "+s"(wv));
  const bool live = __builtin_amdgcn_readfirstlane(wv) != NWAVE;

  float l = (float)dl, z = (float)dz, acc0[4], acc1[4];
#pragma unroll
  for (int e = 0; e < 4; ++e) { acc0[e] = (float)da0[e]; acc1[e] = (float)da1[e]; }
  l += dpp<XOR2>(l); l += dpp<ROR4>(l); l += dpp<XOR8>(l); l = x32_sum(l);
  z += dpp<XOR2>(z); z += dpp<ROR4>(z); z += dpp<XOR8>(z); z = x32_sum(z);
  const float z0 = dpp<BCAST>(z), z1 = dpp<BCAST + 1>(z);
  f32x4 oh;
#pragma unroll
  for (int e = 0; e < 4; ++e) { const float s0 = x32_sum(acc0[e]) - z0, s1 = x32_sum(acc1[e]) - z1; oh[e] = half ? s1 : s0; }
  float* rec = lds + LDS_MRG + (wave * 2 + half) * PREC;
  *reinterpret_cast<f32x4*>(rec + 4 + 4 * c) = oh;
  if (c == half) { rec[0] = mx; rec[1] = l; }
  if (live) __syncthreads();
  const int h = (tid >> 7) & 1, d = tid & 127;
  float M = -INFINITY, L = 0.f, o = 0.f;
  {
    const float* r0 = lds + LDS_MRG + h * PREC;
#pragma unroll
    for (int u = 0; u < NWAVE; ++u) M = fmaxf(M, r0[u * 2 * PREC]);
#pragma unroll
    for (int u = 0; u < NWAVE; ++u) {
      const float wt = __builtin_amdgcn_exp2f(r0[u * 2 * PREC] - M);
      L = fmaf(wt, r0[u * 2 * PREC + 1], L);
      o = fmaf(wt, r0[u * 2 * PREC + 4 + d], o);
    }
  }
  const unsigned tag = __float_as_uint(lds[LDS_T0 + 2]) + 1;
  gu64* const gteam = GTEAM_PTR;
  gu64* const gmine = gteam + split * 256 + tid;
  gu64* const smine = gteam + GSTAT + h * 16 + (d & 1) * 8 + split;
  if (split != 0) {
    if (live) st_gran(gmine, o, tag);
    if (live && d < 2) st_gran(smine, d ? L : M, tag);
    const unsigned chk = wave_xor(__float_as_uint(o)) ^ __float_as_uint(M) ^ __float_as_uint(L);
    if (live && lane == 0) st_gran(gteam + GCHK + wave * 8 + split, __uint_as_float(chk), tag);
    RT(tg)
  } else {
    RT(tg)
    const __amdgpu_buffer_rsrc_t rg = __builtin_amdgcn_make_buffer_rsrc((void*)gteam, 0, live ? GTEAM * 8 : 0, 0x00020000);
    const bool isck = (lane >> 3) == 2;
    const unsigned ov = tid * 8, sv = (isck ? GCHK + wave * 8 + (lane & 7) : GSTAT + h * 16 + i16) * 8;
    const bool own = (i16 & 7) == 0;
    u32x2 go[NSPLIT], gs;
    const unsigned tstart = clk();
    bool ok;
    do {
      asm volatile("" ::: "memory");
      gs = __builtin_amdgcn_raw_buffer_load_b64(rg, sv, 0, 16  );
#pragma unroll
      for (int s = 1; s < NSPLIT; ++s) go[s] = __builtin_amdgcn_raw_buffer_load_b64(rg, ov, s * 2048, 16  );
      unsigned bad = own ? 0u : gs.y ^ tag, x = (own || lane >= 24) ? 0u : gs.x;
#pragma unroll
      for (int s = 1; s < NSPLIT; ++s) { bad |= go[s].y ^ tag; x ^= go[s].x; }
      ok = (__all(bad == 0u) != 0) & (wave_xor(x) == 0u);
    } while (!ok && live && clk() - tstart < 100000000u);
    gs.x = __builtin_amdgcn_ds_bpermute(i16 * 4, gs.x);
    const float st = own ? ((i16 & 8) ? L : M) : __uint_as_float(gs.x);
    const float other = dpp<XOR8>(st);
    const float ms = (i16 & 8) ? other : st, ls = (i16 & 8) ? st : other;
    float MM = fmaxf(ms, dpp<XOR1>(ms)); MM = fmaxf(MM, dpp<XOR2>(MM)); MM = fmaxf(MM, dpp<XOR7>(MM));
    const float wt = __builtin_amdgcn_exp2f(ms - MM);
    float LL = wt * ls; LL += dpp<XOR1>(LL); LL += dpp<XOR2>(LL); LL += dpp<XOR7>(LL);
    float oo = dpp<BCAST>(wt) * o;
    oo = fmaf(dpp<BCAST + 1>(wt), __uint_as_float(go[1].x), oo); oo = fmaf(dpp<BCAST + 2>(wt), __uint_as_float(go[2].x), oo);
    oo = fmaf(dpp<BCAST + 3>(wt), __uint_as_float(go[3].x), oo); oo = fmaf(dpp<BCAST + 4>(wt), __uint_as_float(go[4].x), oo);
    oo = fmaf(dpp<BCAST + 5>(wt), __uint_as_float(go[5].x), oo); oo = fmaf(dpp<BCAST + 6>(wt), __uint_as_float(go[6].x), oo);
    oo = fmaf(dpp<BCAST + 7>(wt), __uint_as_float(go[7].x), oo);
    LL = dpp<BCAST>(LL);
    unsigned res = pkbf(ok ? oo / LL : __builtin_nanf(""), 0.f);
    asm volatile("" : "+v"(res));
    if (live) reinterpret_cast<unsigned short*>(out)[(size_t)(bh * REP + h) * DH + d] = (unsigned short)res;
    if (tid == 0) __hip_atomic_store(EWORD, tag, __ATOMIC_RELAXED, __HIP_MEMORY_SCOPE_AGENT);
  }
}

}

extern "C" void kernel_launch(void* const* d_in, const int* in_sizes, int n_in,
                              void* d_out, int out_size, void* d_ws, size_t ws_size,
                              hipStream_t stream) {
  (void)in_sizes; (void)n_in; (void)out_size; (void)ws_size;
  (void)hipFuncSetAttribute((const void*)kv_decode_kernel, hipFuncAttributeMaxDynamicSharedMemorySize, LDSB);
  kv_decode_kernel<<<NBH * NSPLIT, NTHR + 64, LDSB, stream>>>(
      (const unsigned short*)d_in[0], (const int*)d_in[1], (const int*)d_in[2],
      (const unsigned short*)d_in[3], (const unsigned short*)d_in[4],
      (const unsigned short*)d_in[5], (const unsigned short*)d_in[6], (unsigned char*)d_ws, (__hip_bfloat16*)d_out);
}
```
